```python
import torch

def _softmax(x, axis=-1):
    return torch.softmax(x, dim=axis)
def _gelu(x):
    return torch.nn.functional.gelu(x, approximate="tanh")
def _silu(x):
    return torch.nn.functional.silu(x)
def _sigmoid(x):
    return torch.sigmoid(x)
def _layernorm(x, axis=-1, eps=1e-5):
    m = x.mean(dim=axis, keepdim=True)
    v = x.var(dim=axis, keepdim=True, unbiased=False)
    return (x - m) / torch.sqrt(v + eps)
def _scatter_add(tgt, idx, src, axis=0):
    return tgt.clone().index_add_(axis, idx, src)
def _segment_sum(src, seg, n):
    out = torch.zeros((n,) + src.shape[1:], dtype=src.dtype)
    return out.index_add_(0, seg, src)
def _pad(x, pw):
    flat = tuple(v for pair in reversed(pw) for v in pair)
    return torch.nn.functional.pad(x, flat)

kernel_name = 'parallel_sum__softmax_full_mha__moe_arch_mod__prompt'

def setup_inputs():

    _w = (lambda a: (lambda t: t.to(torch.bfloat16) if t.is_floating_point() else t)(torch.as_tensor(a)))
    def _A_setup():
        B, S, D = 1, 2048, 512
        return {
            "x": _w((lambda *s: torch.randn(*s, dtype=torch.bfloat16) * 0.05)(B, S, D)),
            "wqkv": _w((lambda *s: torch.randn(*s, dtype=torch.bfloat16) * 0.2)(D, 3 * D)),
            "wo": _w((lambda *s: torch.randn(*s, dtype=torch.bfloat16) * 0.2)(D, D)),
        }
    _A_in = _A_setup()

    def _B_setup():
        B, S, D, F, C = 1, 2048, 512, 1376, 8
        return {
            "x": _w((lambda *s: torch.randn(*s, dtype=torch.bfloat16) * 0.05)(B, S, D)),
            "w_r": _w((lambda *s: torch.randn(*s, dtype=torch.bfloat16) * 1.6)(D)),
            "w_gate": _w((lambda *s: torch.randn(*s, dtype=torch.bfloat16) * 0.2)(D, F)),
            "w_up": _w((lambda *s: torch.randn(*s, dtype=torch.bfloat16) * 0.2)(D, F)),
            "w_down": _w((lambda *s: torch.randn(*s, dtype=torch.bfloat16) * 0.2)(F, D)),
        }
    _B_in = _B_setup()

    return {
        "A_x": _A_in["x"],
        "A_wqkv": _A_in["wqkv"],
        "A_wo": _A_in["wo"],
        "B_x": _B_in["x"],
        "B_w_r": _B_in["w_r"],
        "B_w_gate": _B_in["w_gate"],
        "B_w_up": _B_in["w_up"],
        "B_w_down": _B_in["w_down"],
    }

def reference(A_x, A_wqkv, A_wo, B_x, B_w_r, B_w_gate, B_w_up, B_w_down):
    def _A_ref(x, wqkv, wo):
        B, S, D = x.shape
        H = 32; Dh = D // H
        qkv = (lambda x, *d: x.permute(*d))((x @ wqkv).reshape(B, S, 3, H, Dh), 2, 0, 3, 1, 4)
        q, k, v = qkv[0], qkv[1], qkv[2]
        scale = Dh ** -0.5
        scores = torch.einsum("bhqd,bhkd->bhqk", q, k) * scale
        p = _softmax(scores, axis=-1)
        o = torch.einsum("bhqk,bhkd->bhqd", p, v)
        return (lambda x, *d: x.permute(*d))(o, 0, 2, 1, 3).reshape(B, S, D) @ wo

    def _B_ref(x, w_r, w_gate, w_up, w_down):
        B, S, D = x.shape
        C = 8
        r = x @ w_r
        idx = torch.argsort(-r, axis=-1)[:, :C]
        idx_e = (lambda x, n, axis: torch.repeat_interleave(x, n, dim=axis))(idx.reshape(B, C, 1), D, axis=2)
        x_sel = (lambda x, i, axis: torch.take_along_dim(x, i, dim=axis))(x, idx_e, axis=1)
        g = _silu(x_sel @ w_gate)
        u = x_sel @ w_up
        y = (g * u) @ w_down
        r_sel = _sigmoid((lambda x, i, axis: torch.take_along_dim(x, i, dim=axis))(r, idx, axis=1))
        y = y * r_sel.reshape(B, C, 1)
        b_off = (torch.arange(B) * S).reshape(B, 1)
        flat_idx = (idx + b_off).reshape(B * C)
        zero = (x * 0.0).reshape(B * S, D)
        upd = _scatter_add(zero, flat_idx, y.reshape(B * C, D), axis=0)
        return x + upd.reshape(B, S, D)

    x = A_x
    xn = _layernorm(x, axis=-1)
    return x + _A_ref(xn, A_wqkv, A_wo) + _B_ref(xn, B_w_r, B_w_gate, B_w_up, B_w_down)
```

```cpp
#include <hip/hip_runtime.h>
#include <hip/hip_bf16.h>
#include <stdint.h>

typedef __attribute__((ext_vector_type(8))) __bf16 bf16x8;
typedef __attribute__((ext_vector_type(2))) __bf16 bf16x2;
typedef __attribute__((ext_vector_type(2))) float f32x2;
typedef __attribute__((ext_vector_type(4))) short s16x4;
typedef __attribute__((ext_vector_type(8))) short s16x8;
typedef __attribute__((ext_vector_type(16))) float f32x16;
typedef __attribute__((ext_vector_type(4))) unsigned u32x4;
typedef __attribute__((ext_vector_type(4))) float f32x4;

#define DI __device__ __forceinline__
#define MFMA32(a, b, c) __builtin_amdgcn_mfma_f32_32x32x16_bf16((a), (b), (c), 0, 0, 0)

constexpr int SEQ = 2048, DM = 512, NH = 32, FF = 1376, NSEL = 8;
constexpr int VHEAD_BYTES = 128 * 544;
constexpr int FCH = 32;
constexpr int NFCH = FF / FCH;

constexpr size_t WS_XN = 0;
constexpr size_t WS_R = WS_XN + (size_t)SEQ * DM * 2;
constexpr size_t WS_Q = WS_R + (size_t)SEQ * 4;
constexpr size_t WS_K = WS_Q + (size_t)SEQ * DM * 2;
constexpr size_t WS_V = WS_K + (size_t)SEQ * DM * 2;
constexpr size_t WS_O = WS_V + (size_t)NH * VHEAD_BYTES + 1024;
constexpr size_t WS_WOT = WS_O + (size_t)SEQ * DM * 2;
constexpr size_t WS_YP = WS_WOT + (size_t)DM * DM * 2;
constexpr size_t WS_SEL = WS_YP + (size_t)NFCH * NSEL * DM * 4;

DI float bf2f(unsigned short u) { return __uint_as_float((unsigned)u << 16); }
DI unsigned pk2(float a, float b) {
  f32x2 v = {a, b};
  bf16x2 r = __builtin_convertvector(v, bf16x2);
  return __builtin_bit_cast(unsigned, r);
}
DI float bfr(float a) { return __uint_as_float(pk2(0.f, a) & 0xffff0000u); }
DI unsigned short f2bf(float a) { return (unsigned short)(pk2(a, 0.f) & 0xffffu); }
DI bf16x8 pack8(const f32x16& x, int s) {
  u32x4 p;
  p[0] = pk2(x[8 * s + 0], x[8 * s + 1]);
  p[1] = pk2(x[8 * s + 2], x[8 * s + 3]);
  p[2] = pk2(x[8 * s + 4], x[8 * s + 5]);
  p[3] = pk2(x[8 * s + 6], x[8 * s + 7]);
  return __builtin_bit_cast(bf16x8, p);
}
DI float wave_sum(float v) {
#pragma unroll
  for (int o = 32; o > 0; o >>= 1) v += __shfl_xor(v, o);
  return v;
}

constexpr int K1_APITCH = 1040;
constexpr int K1_WPITCH = 320;
__global__ __launch_bounds__(256, 2) void k1_ln_qkv(const unsigned short* __restrict__ x,
                                                    const unsigned short* __restrict__ wqkv,
                                                    const unsigned short* __restrict__ w_r,
                                                    char* __restrict__ ws) {
  __shared__ __attribute__((aligned(16))) char smem[64 * K1_APITCH + 32 * K1_WPITCH];
  char* Aimg = smem;
  char* Wimg = smem + 64 * K1_APITCH;
  const int tid = threadIdx.x, lane = tid & 63, wv = tid >> 6;
  const int nt = blockIdx.x % 12, mt = blockIdx.x / 12;
  const int m0 = mt * 64, n0 = nt * 128;
  const int r31 = lane & 31, h = lane >> 5;

  u32x4 wreg[2];
  auto load_w = [&](int kc) {
#pragma unroll
    for (int i = 0; i < 2; ++i) {
      int p = tid + 256 * i;
      int row = p >> 4, c8 = p & 15;
      wreg[i] = *(const u32x4*)(wqkv + (size_t)(kc * 32 + row) * 1536 + n0 + c8 * 8);
    }
  };
  load_w(0);

  {
    float wr[8];
    if (nt == 0) {
      s16x8 w8 = *(const s16x8*)(w_r + lane * 8);
#pragma unroll
      for (int j = 0; j < 8; ++j) wr[j] = bf2f((unsigned short)w8[j]);
    }
    for (int i = 0; i < 16; ++i) {
      const int row = wv * 16 + i;
      s16x8 v8 = *(const s16x8*)(x + (size_t)(m0 + row) * DM + lane * 8);
      float v[8];
      float s = 0.f;
#pragma unroll
      for (int j = 0; j < 8; ++j) { v[j] = bf2f((unsigned short)v8[j]); s += v[j]; }
      s = wave_sum(s);
      const float mean = s * (1.0f / DM);
      float q = 0.f;
#pragma unroll
      for (int j = 0; j < 8; ++j) { float d = v[j] - mean; q += d * d; }
      q = wave_sum(q);
      const float var_b = bfr(q * (1.0f / DM));
      const float mean_b = bfr(mean);
      const float sd = bfr(sqrtf(bfr(var_b + 1e-5f)));
      float xn[8];
#pragma unroll
      for (int j = 0; j < 8; ++j) xn[j] = bfr(bfr(v[j] - mean_b) / sd);
      u32x4 pkd;
#pragma unroll
      for (int j = 0; j < 4; ++j) pkd[j] = pk2(xn[2 * j], xn[2 * j + 1]);
      *(u32x4*)(Aimg + row * K1_APITCH + lane * 16) = pkd;
      if (nt == 0) {
        *(u32x4*)(ws + WS_XN + ((size_t)(m0 + row) * DM + lane * 8) * 2) = pkd;
        float rr = 0.f;
#pragma unroll
        for (int j = 0; j < 8; ++j) rr += xn[j] * wr[j];
        rr = wave_sum(rr);
        if (lane == 0) ((float*)(ws + WS_R))[m0 + row] = bfr(rr);
      }
    }
  }

  const bool isV = nt >= 8;
  f32x16 acc0, acc1;
#pragma unroll
  for (int i = 0; i < 16; ++i) { acc0[i] = 0.f; acc1[i] = 0.f; }
  const int q4 = (lane & 15) >> 2, p4 = lane & 3, blk = (lane >> 4) & 1;
  for (int kc = 0; kc < 16; ++kc) {
    __syncthreads();
#pragma unroll
    for (int i = 0; i < 2; ++i) {
      int p = tid + 256 * i;
      int row = p >> 4, c8 = p & 15;
      *(u32x4*)(Wimg + row * K1_WPITCH + c8 * 16) = wreg[i];
    }
    __syncthreads();
    if (kc + 1 < 16) load_w(kc + 1);
#pragma unroll
    for (int s = 0; s < 2; ++s) {
      const int kb = (kc * 32 + 16 * s + 8 * h) * 2;
      bf16x8 a0 = *(const bf16x8*)(Aimg + r31 * K1_APITCH + kb);
      bf16x8 a1 = *(const bf16x8*)(Aimg + (32 + r31) * K1_APITCH + kb);
      const char* wb = Wimg + (16 * s + 8 * h + q4) * K1_WPITCH + (wv * 32 + blk * 16 + 4 * p4) * 2;
      s16x4 lo = __builtin_amdgcn_ds_read_tr16_b64_v4i16((__attribute__((address_space(3))) s16x4*)(wb));
      s16x4 hi = __builtin_amdgcn_ds_read_tr16_b64_v4i16((__attribute__((address_space(3))) s16x4*)(wb + 4 * K1_WPITCH));
      s16x8 b8 = __builtin_shufflevector(lo, hi, 0, 1, 2, 3, 4, 5, 6, 7);
      bf16x8 b = __builtin_bit_cast(bf16x8, b8);
      if (isV) { acc0 = MFMA32(a0, b, acc0); acc1 = MFMA32(a1, b, acc1); }
      else     { acc0 = MFMA32(b, a0, acc0); acc1 = MFMA32(b, a1, acc1); }
    }
  }
  const int col0 = n0 + wv * 32;
  if (!isV) {
    char* img = ws + (nt < 4 ? WS_Q : WS_K);
    const int head = (col0 & 511) >> 4;
#pragma unroll
    for (int sub = 0; sub < 2; ++sub) {
      const f32x16& acc = sub ? acc1 : acc0;
      const int tok = m0 + sub * 32 + r31;
#pragma unroll
      for (int hh = 0; hh < 2; ++hh) {
        bf16x8 v = pack8(acc, hh);
        *(bf16x8*)(img + ((size_t)((head + hh) * 2 + h) * SEQ + tok) * 16) = v;
      }
    }
  } else {
    const int head = ((col0 - 1024) >> 4) + (r31 >> 4), d = r31 & 15;
    char* img = ws + WS_V + (size_t)head * VHEAD_BYTES;
#pragma unroll
    for (int sub = 0; sub < 2; ++sub) {
      const f32x16& acc = sub ? acc1 : acc0;
#pragma unroll
      for (int gg = 0; gg < 2; ++gg) {
        const int g = ((m0 + sub * 32) >> 4) + gg;
        bf16x8 v = pack8(acc, gg);
        *(bf16x8*)(img + g * 544 + h * 272 + d * 16) = v;
        if (d == 0) {
          u32x4 ones = {0x3f803f80u, 0x3f803f80u, 0x3f803f80u, 0x3f803f80u};
          *(u32x4*)(img + g * 544 + h * 272 + 256) = ones;
        }
      }
    }
  }
}

constexpr int K2_KBYTES = 2 * SEQ * 16;
constexpr int K2_LDS = K2_KBYTES + VHEAD_BYTES + 512;
constexpr int K2_NSIDE = NFCH + 8;

DI void block_top8(const float* __restrict__ r, char* smem, int tid, int* sel, float* selv) {
  float* cv = (float*)smem;
  int* ci = (int*)(smem + 64);
  float v0 = r[tid], v1 = r[tid + 1024];
  int i0 = tid, i1 = tid + 1024;
  const int lane = tid & 63, wv = tid >> 6;
  for (int it = 0; it < NSEL; ++it) {
    float bv; int bi;
    if (v1 > v0) { bv = v1; bi = i1; } else { bv = v0; bi = i0; }
#pragma unroll
    for (int o = 32; o > 0; o >>= 1) {
      float ov = __shfl_xor(bv, o); int oi = __shfl_xor(bi, o);
      if (ov > bv || (ov == bv && oi < bi)) { bv = ov; bi = oi; }
    }
    if (lane == 0) { cv[wv] = bv; ci[wv] = bi; }
    __syncthreads();
    float fv = cv[0]; int fi = ci[0];
#pragma unroll
    for (int k = 1; k < 16; ++k) { float ov = cv[k]; int oi = ci[k]; if (ov > fv || (ov == fv && oi < fi)) { fv = ov; fi = oi; } }
    sel[it] = fi; selv[it] = fv;
    if (i0 == fi) v0 = -INFINITY;
    if (i1 == fi) v1 = -INFINITY;
    __syncthreads();
  }
}

__global__ __launch_bounds__(1024) void k2_attn(const unsigned short* __restrict__ w_gate,
                                                const unsigned short* __restrict__ w_up,
                                                const unsigned short* __restrict__ w_down,
                                                const unsigned short* __restrict__ wo,
                                                char* __restrict__ ws) {
  __shared__ __attribute__((aligned(16))) char smem[K2_LDS];
  const int tid = threadIdx.x, lane = tid & 63, wv = tid >> 6;
  const int r31 = lane & 31, h = lane >> 5;
  const int bid = blockIdx.x;

  if (bid < NFCH) {
    const int f0 = bid * FCH;
    int sel[NSEL]; float selv[NSEL];
    block_top8((const float*)(ws + WS_R), smem, tid, sel, selv);
    float* xs = (float*)(smem + 256);
    float* red = (float*)(smem + 256 + 16384);
    float* hh = (float*)(smem + 256 + 16384 + 32768);
    for (int i = tid; i < NSEL * DM; i += 1024) {
      int row = i >> 9, k = i & 511;
      xs[i] = bf2f(((const unsigned short*)(ws + WS_XN))[(size_t)sel[row] * DM + k]);
    }
    if (bid == 0 && tid < NSEL) {
      ((int*)(ws + WS_SEL))[tid] = sel[tid];
      float sg = 1.0f / (1.0f + __expf(-selv[tid]));
      ((float*)(ws + WS_SEL + 32))[tid] = bfr(sg);
    }
    __syncthreads();
    {
      const int cp = tid & 15, ks = tid >> 4;
      float ag[NSEL][2], au[NSEL][2];
#pragma unroll
      for (int i = 0; i < NSEL; ++i) { ag[i][0] = ag[i][1] = au[i][0] = au[i][1] = 0.f; }
#pragma unroll
      for (int kk = 0; kk < 8; ++kk) {
        const int k = ks * 8 + kk;
        unsigned g2 = *(const unsigned*)(w_gate + (size_t)k * FF + f0 + cp * 2);
        unsigned u2 = *(const unsigned*)(w_up + (size_t)k * FF + f0 + cp * 2);
        float g0 = __uint_as_float(g2 << 16), g1 = __uint_as_float(g2 & 0xffff0000u);
        float u0 = __uint_as_float(u2 << 16), u1 = __uint_as_float(u2 & 0xffff0000u);
#pragma unroll
        for (int i = 0; i < NSEL; ++i) {
          float xv = xs[i * DM + k];
          ag[i][0] += xv * g0; ag[i][1] += xv * g1; au[i][0] += xv * u0; au[i][1] += xv * u1;
        }
      }
#pragma unroll
      for (int i = 0; i < NSEL; ++i)
#pragma unroll
        for (int c = 0; c < 2; ++c) {
          ag[i][c] += __shfl_xor(ag[i][c], 16); ag[i][c] += __shfl_xor(ag[i][c], 32);
          au[i][c] += __shfl_xor(au[i][c], 16); au[i][c] += __shfl_xor(au[i][c], 32);
        }
      if (lane < 16) {
#pragma unroll
        for (int i = 0; i < NSEL; ++i)
#pragma unroll
          for (int c = 0; c < 2; ++c) {
            red[((wv * 2 + 0) * NSEL + i) * FCH + cp * 2 + c] = ag[i][c];
            red[((wv * 2 + 1) * NSEL + i) * FCH + cp * 2 + c] = au[i][c];
          }
      }
    }
    __syncthreads();
    if (tid < NSEL * FCH) {
      float g = 0.f, u = 0.f;
#pragma unroll
      for (int w = 0; w < 16; ++w) { g += red[(w * 2 + 0) * NSEL * FCH + tid]; u += red[(w * 2 + 1) * NSEL * FCH + tid]; }
      g = bfr(g); u = bfr(u);
      float sl = bfr(g / (1.0f + __expf(-g)));
      hh[tid] = bfr(sl * u);
    }
    __syncthreads();
    {
      const int n = tid & 511, rh = tid >> 9;
      float y[4] = {0.f, 0.f, 0.f, 0.f};
#pragma unroll 8
      for (int f = 0; f < FCH; ++f) {
        float wd = bf2f(w_down[(size_t)(f0 + f) * DM + n]);
#pragma unroll
        for (int i = 0; i < 4; ++i) y[i] += hh[(rh * 4 + i) * FCH + f] * wd;
      }
#pragma unroll
      for (int i = 0; i < 4; ++i) ((float*)(ws + WS_YP))[((size_t)bid * NSEL + rh * 4 + i) * DM + n] = y[i];
    }
    return;
  }
  if (bid < K2_NSIDE) {
    const int tb = bid - NFCH;
    unsigned short* tile = (unsigned short*)smem;
    for (int i = tid; i < 512 * 8; i += 1024) {
      int k = i >> 3, c8 = i & 7;
      s16x8 v = *(const s16x8*)(wo + (size_t)k * DM + tb * 64 + c8 * 8);
#pragma unroll
      for (int j = 0; j < 8; ++j) tile[k * 66 + c8 * 8 + j] = (unsigned short)v[j];
    }
    __syncthreads();
    for (int i = tid; i < 64 * 64; i += 1024) {
      int n = i >> 6, pc = i & 63;
      int head = pc >> 1, hh2 = pc & 1;
      s16x8 v;
#pragma unroll
      for (int j = 0; j < 8; ++j) v[j] = (short)tile[(head * 16 + 8 * (j >> 2) + 4 * hh2 + (j & 3)) * 66 + n];
      *(s16x8*)(ws + WS_WOT + ((size_t)(tb * 64 + n) * DM + pc * 8) * 2) = v;
    }
    return;
  }

  const int ab = bid - K2_NSIDE;
  const int head = (ab & 7) * 4 + ((ab >> 3) & 3), qb = ab >> 5;
  char* Kl = smem;
  char* Vl = smem + K2_KBYTES;
  {
    const char* kg = ws + WS_K + (size_t)head * K2_KBYTES;
    const char* vg = ws + WS_V + (size_t)head * VHEAD_BYTES;
#pragma unroll
    for (int i = 0; i < 4; ++i) *(u32x4*)(Kl + (tid + 1024 * i) * 16) = *(const u32x4*)(kg + (tid + 1024 * i) * 16);
#pragma unroll
    for (int i = 0; i < 5; ++i) {
      int off = (tid + 1024 * i) * 16;
      if (off < VHEAD_BYTES) *(u32x4*)(Vl + off) = *(const u32x4*)(vg + off);
    }
  }
  const int qt = wv & 7, half = wv >> 3;
  const int q0 = qb * 256 + qt * 32;
  const bf16x8 qf = *(const bf16x8*)(ws + WS_Q + ((size_t)(head * 2 + h) * SEQ + q0 + r31) * 16);
  __syncthreads();

  const float cs = 0.25f * 1.4426950408889634f;
  f32x16 o;
#pragma unroll
  for (int i = 0; i < 16; ++i) o[i] = 0.f;
  float m = -INFINITY;
  f32x16 zero;
#pragma unroll
  for (int i = 0; i < 16; ++i) zero[i] = 0.f;

  for (int grp = 0; grp < 8; ++grp) {
    const int key0 = half * 1024 + grp * 128;
    f32x16 s[4];
#pragma unroll
    for (int t = 0; t < 4; ++t) {
      bf16x8 kf = *(const bf16x8*)(Kl + h * 32768 + (key0 + t * 32 + r31) * 16);
      s[t] = MFMA32(kf, qf, zero);
    }
    float mx = s[0][0];
#pragma unroll
    for (int t = 0; t < 4; ++t)
#pragma unroll
      for (int i = 0; i < 16; ++i) mx = fmaxf(mx, s[t][i]);
    {
      auto sw = __builtin_amdgcn_permlane32_swap(__float_as_uint(mx), __float_as_uint(mx), false, false);
      mx = fmaxf(__uint_as_float(sw[0]), __uint_as_float(sw[1]));
    }
    mx = bfr(mx);
    const float mnew = fmaxf(m, mx);
    const float alpha = __builtin_amdgcn_exp2f((m - mnew) * cs);
    m = mnew;
    const float nm = -mnew * cs;
#pragma unroll
    for (int i = 0; i < 9; ++i) o[i] *= alpha;
#pragma unroll
    for (int t = 0; t < 4; ++t) {
      f32x16 p;
#pragma unroll
      for (int i = 0; i < 16; ++i) p[i] = __builtin_amdgcn_exp2f(fmaf(bfr(s[t][i]), cs, nm));
      const int g = (key0 + t * 32) >> 4;
      bf16x8 v0 = *(const bf16x8*)(Vl + g * 544 + h * 272 + r31 * 16);
      bf16x8 v1 = *(const bf16x8*)(Vl + (g + 1) * 544 + h * 272 + r31 * 16);
      o = MFMA32(v0, pack8(p, 0), o);
      o = MFMA32(v1, pack8(p, 1), o);
    }
  }
  __syncthreads();
  float* mb = (float*)smem;
  if (half == 1) {
#pragma unroll
    for (int i = 0; i < 9; ++i) mb[(qt * 10 + i) * 64 + lane] = o[i];
    mb[(qt * 10 + 9) * 64 + lane] = m;
  }
  __syncthreads();
  if (half == 0) {
    const float m2 = mb[(qt * 10 + 9) * 64 + lane];
    const float mm = fmaxf(m, m2);
    const float a1 = __builtin_amdgcn_exp2f((m - mm) * cs), a2 = __builtin_amdgcn_exp2f((m2 - mm) * cs);
#pragma unroll
    for (int i = 0; i < 9; ++i) o[i] = o[i] * a1 + mb[(qt * 10 + i) * 64 + lane] * a2;
    auto sw = __builtin_amdgcn_permlane32_swap(__float_as_uint(o[8]), __float_as_uint(o[8]), false, false);
    const float l = __uint_as_float(sw[0]);
    const float inv = 1.0f / l;
#pragma unroll
    for (int i = 0; i < 8; ++i) o[i] *= inv;
    bf16x8 ov = pack8(o, 0);
    *(bf16x8*)(ws + WS_O + ((size_t)(q0 + r31) * DM + head * 16 + h * 8) * 2) = ov;
  }
}

__global__ __launch_bounds__(256) void k3_out(const unsigned short* __restrict__ x,
                                              const char* __restrict__ ws,
                                              unsigned short* __restrict__ out) {
  __shared__ int slot[64];
  const int tid = threadIdx.x, lane = tid & 63, wv = tid >> 6;
  const int r31 = lane & 31, h = lane >> 5;
  const int mt = blockIdx.x >> 3, nt = blockIdx.x & 7;
  const int m0 = mt * 64 + (wv >> 1) * 32, n0 = nt * 64 + (wv & 1) * 32;
  if (tid < 64) slot[tid] = -1;
  __syncthreads();
  if (tid < NSEL) { int idx = ((const int*)(ws + WS_SEL))[tid]; if ((idx >> 6) == mt) slot[idx & 63] = tid; }
  __syncthreads();
  const char* Ab = ws + WS_O + ((size_t)(m0 + r31) * DM + 8 * h) * 2;
  const char* Bb = ws + WS_WOT + ((size_t)(n0 + r31) * DM + 8 * h) * 2;
  f32x16 acc;
#pragma unroll
  for (int i = 0; i < 16; ++i) acc[i] = 0.f;
#pragma unroll 8
  for (int ks = 0; ks < 32; ++ks) {
    bf16x8 a = *(const bf16x8*)(Ab + ks * 32);
    bf16x8 b = *(const bf16x8*)(Bb + ks * 32);
    acc = MFMA32(a, b, acc);
  }
  const int n = n0 + r31;
  const unsigned short* xn = (const unsigned short*)(ws + WS_XN);
#pragma unroll
  for (int i = 0; i < 16; ++i) {
    const int lr = (wv >> 1) * 32 + (i & 3) + 8 * (i >> 2) + 4 * h;
    const int tok = mt * 64 + lr;
    const float xa = bfr(bf2f(x[(size_t)tok * DM + n]) + bfr(acc[i]));
    float b = bf2f(xn[(size_t)tok * DM + n]);
    const int sl = slot[lr];
    if (sl >= 0) {
      float y = 0.f;
      for (int c = 0; c < NFCH; ++c) y += ((const float*)(ws + WS_YP))[((size_t)c * NSEL + sl) * DM + n];
      const float sg = ((const float*)(ws + WS_SEL + 32))[sl];
      b = bfr(b + bfr(bfr(y) * sg));
    }
    out[(size_t)tok * DM + n] = f2bf(xa + b);
  }
}

extern "C" void kernel_launch(void* const* d_in, const int* in_sizes, int n_in,
                              void* d_out, int out_size, void* d_ws, size_t ws_size,
                              hipStream_t stream) {
  const unsigned short* x = (const unsigned short*)d_in[0];
  const unsigned short* wqkv = (const unsigned short*)d_in[1];
  const unsigned short* wo = (const unsigned short*)d_in[2];
  const unsigned short* w_r = (const unsigned short*)d_in[4];
  const unsigned short* w_gate = (const unsigned short*)d_in[5];
  const unsigned short* w_up = (const unsigned short*)d_in[6];
  const unsigned short* w_down = (const unsigned short*)d_in[7];
  char* ws = (char*)d_ws;
  k1_ln_qkv<<<12 * 32, 256, 0, stream>>>(x, wqkv, w_r, ws);
  k2_attn<<<K2_NSIDE + 256, 1024, 0, stream>>>(w_gate, w_up, w_down, wo, ws);
  k3_out<<<256, 256, 0, stream>>>(x, ws, (unsigned short*)d_out);
}
```

```cpp
#include <hip/hip_runtime.h>
#include <hip/hip_bf16.h>
#include <stdint.h>

typedef __attribute__((ext_vector_type(8))) __bf16 bf16x8;
typedef __attribute__((ext_vector_type(2))) __bf16 bf16x2;
typedef __attribute__((ext_vector_type(2))) float f32x2;
typedef __attribute__((ext_vector_type(4))) short s16x4;
typedef __attribute__((ext_vector_type(8))) short s16x8;
typedef __attribute__((ext_vector_type(16))) float f32x16;
typedef __attribute__((ext_vector_type(4))) unsigned u32x4;
typedef __attribute__((ext_vector_type(2))) unsigned u32x2;
typedef __attribute__((ext_vector_type(4))) float f32x4;

#define DI __device__ __forceinline__
#define MFMA32(a, b, c) __builtin_amdgcn_mfma_f32_32x32x16_bf16((a), (b), (c), 0, 0, 0)

constexpr int SEQ = 2048, DM = 512, NH = 32, FF = 1376, NSEL = 8;
constexpr int VHEAD_BYTES = 128 * 544;
constexpr int FCH = 16;
constexpr int NFCH = FF / FCH;

constexpr size_t WS_XN = 0;
constexpr size_t WS_R = WS_XN + (size_t)SEQ * DM * 2;
constexpr size_t WS_Q = WS_R + (size_t)SEQ * 4;
constexpr size_t WS_K = WS_Q + (size_t)SEQ * DM * 2;
constexpr size_t WS_V = WS_K + (size_t)SEQ * DM * 2;
constexpr size_t WS_O = WS_V + (size_t)NH * VHEAD_BYTES + 1024;
constexpr size_t WS_WOT = WS_O + (size_t)SEQ * DM * 2;
constexpr size_t WS_YP = WS_WOT + (size_t)DM * DM * 2;
constexpr size_t WS_SEL = WS_YP + (size_t)NFCH * NSEL * DM * 4;

DI float bf2f(unsigned short u) { return __uint_as_float((unsigned)u << 16); }
DI float bflo(unsigned u) { return __uint_as_float(u << 16); }
DI float bfhi(unsigned u) { return __uint_as_float(u & 0xffff0000u); }
DI unsigned pk2(float a, float b) {
  f32x2 v = {a, b};
  bf16x2 r = __builtin_convertvector(v, bf16x2);
  return __builtin_bit_cast(unsigned, r);
}
DI float bfr(float a) { float r; asm("v_cvt_pk_bf16_f32 %0, 0, %1" : "=v"(r) : "v"(a)); return r; }
DI unsigned short f2bf(float a) { return (unsigned short)(pk2(a, 0.f) & 0xffffu); }
DI bf16x8 pack8(const f32x16& x, int s) {
  u32x4 p;
  p[0] = pk2(x[8 * s + 0], x[8 * s + 1]);
  p[1] = pk2(x[8 * s + 2], x[8 * s + 3]);
  p[2] = pk2(x[8 * s + 4], x[8 * s + 5]);
  p[3] = pk2(x[8 * s + 6], x[8 * s + 7]);
  return __builtin_bit_cast(bf16x8, p);
}
#define DPP_ADD(v, ctrl) v += __uint_as_float(__builtin_amdgcn_update_dpp(0u, __float_as_uint(v), ctrl, 0xf, 0xf, true))
DI float wave_sum(float v) {
  DPP_ADD(v, 0x111);
  DPP_ADD(v, 0x112);
  DPP_ADD(v, 0x114);
  DPP_ADD(v, 0x118);
  v += __uint_as_float(__builtin_amdgcn_update_dpp(0u, __float_as_uint(v), 0x142, 0xa, 0xf, true));
  v += __uint_as_float(__builtin_amdgcn_update_dpp(0u, __float_as_uint(v), 0x143, 0xc, 0xf, true));
  return __uint_as_float(__builtin_amdgcn_readlane(__float_as_uint(v), 63));
}

__global__ __launch_bounds__(256) void k0_ln(const unsigned short* __restrict__ x,
                                             const unsigned short* __restrict__ w_r,
                                             char* __restrict__ ws) {
  const int tid = threadIdx.x, lane = tid & 63, wv = tid >> 6;
  const int row0 = blockIdx.x * 8 + wv * 2;
  u32x4 xr[2];
#pragma unroll
  for (int i = 0; i < 2; ++i) xr[i] = *(const u32x4*)(x + (size_t)(row0 + i) * DM + lane * 8);
  const u32x4 w4 = *(const u32x4*)(w_r + lane * 8);
#pragma unroll
  for (int i = 0; i < 2; ++i) {
    float v[8];
#pragma unroll
    for (int j = 0; j < 4; ++j) { v[2 * j] = bflo(xr[i][j]); v[2 * j + 1] = bfhi(xr[i][j]); }
    float s = 0.f;
#pragma unroll
    for (int j = 0; j < 8; ++j) s += v[j];
    s = wave_sum(s);
    const float mean = s * (1.0f / DM);
    float q = 0.f;
#pragma unroll
    for (int j = 0; j < 8; ++j) { float d = v[j] - mean; q = fmaf(d, d, q); }
    q = wave_sum(q);
    const float var_b = bfr(q * (1.0f / DM));
    const float mean_b = bfr(mean);
    const float sd = bfr(sqrtf(bfr(var_b + 1e-5f)));
    const float rinv = 1.0f / sd;
    float xn[8];
#pragma unroll
    for (int j = 0; j < 8; ++j) xn[j] = bfr(bfr(v[j] - mean_b) * rinv);
    u32x4 pkd;
#pragma unroll
    for (int j = 0; j < 4; ++j) pkd[j] = pk2(xn[2 * j], xn[2 * j + 1]);
    *(u32x4*)(ws + WS_XN + ((size_t)(row0 + i) * DM + lane * 8) * 2) = pkd;
    float rr = 0.f;
#pragma unroll
    for (int j = 0; j < 4; ++j) { rr = fmaf(xn[2 * j], bflo(w4[j]), rr); rr = fmaf(xn[2 * j + 1], bfhi(w4[j]), rr); }
    rr = wave_sum(rr);
    if (lane == 0) ((float*)(ws + WS_R))[row0 + i] = bfr(rr);
  }
}

constexpr int K1_XP = 144;
constexpr int K1_WP = 320;
constexpr int K1_XB = 128 * K1_XP;
constexpr int K1_WB = 64 * K1_WP;
constexpr int K1_LDS = 2 * (K1_XB + K1_WB);
constexpr int K1_NGEMM = 192;

template <bool ISV>
DI void k1_gemm_body(const unsigned short* __restrict__ wqkv, char* __restrict__ ws, char* smem, int mt, int nt) {
  const int tid = threadIdx.x, lane = tid & 63, wv = tid >> 6;
  const int r31 = lane & 31, h = lane >> 5;
  const int wm = wv >> 1, wn = wv & 1;
  const int m0 = mt * 128, n0 = nt * 128;
  const unsigned short* xn = (const unsigned short*)(ws + WS_XN);
  u32x4 xr[4], wr[4];
  auto issue = [&](int c) {
#pragma unroll
    for (int i = 0; i < 4; ++i) {
      int p = tid + 256 * i;
      xr[i] = *(const u32x4*)(xn + (size_t)(m0 + (p >> 3)) * DM + c * 64 + (p & 7) * 8);
      wr[i] = *(const u32x4*)(wqkv + (size_t)(c * 64 + (p >> 4)) * 1536 + n0 + (p & 15) * 8);
    }
  };
  auto commit = [&](int b) {
    char* X = smem + b * (K1_XB + K1_WB);
    char* W = X + K1_XB;
#pragma unroll
    for (int i = 0; i < 4; ++i) {
      int p = tid + 256 * i;
      *(u32x4*)(X + (p >> 3) * K1_XP + (p & 7) * 16) = xr[i];
      *(u32x4*)(W + (p >> 4) * K1_WP + (p & 15) * 16) = wr[i];
    }
  };
  f32x16 acc[2][2];
#pragma unroll
  for (int a = 0; a < 2; ++a)
#pragma unroll
    for (int b = 0; b < 2; ++b)
#pragma unroll
      for (int i = 0; i < 16; ++i) acc[a][b][i] = 0.f;
  const int q4 = (lane & 15) >> 2, p4 = lane & 3, blk = (lane >> 4) & 1;
  issue(0);
  commit(0);
  __syncthreads();
  for (int c = 0; c < 8; ++c) {
    if (c + 1 < 8) issue(c + 1);
    const char* X = smem + (c & 1) * (K1_XB + K1_WB);
    const char* W = X + K1_XB;
#pragma unroll
    for (int s = 0; s < 4; ++s) {
      bf16x8 a[2], b[2];
#pragma unroll
      for (int i = 0; i < 2; ++i) {
        a[i] = *(const bf16x8*)(X + (wm * 64 + i * 32 + r31) * K1_XP + (16 * s + 8 * h) * 2);
        const char* wb = W + (16 * s + 8 * h + q4) * K1_WP + (wn * 64 + i * 32 + blk * 16 + 4 * p4) * 2;
        s16x4 lo = __builtin_amdgcn_ds_read_tr16_b64_v4i16((__attribute__((address_space(3))) s16x4*)(wb));
        s16x4 hi = __builtin_amdgcn_ds_read_tr16_b64_v4i16((__attribute__((address_space(3))) s16x4*)(wb + 4 * K1_WP));
        s16x8 b8 = __builtin_shufflevector(lo, hi, 0, 1, 2, 3, 4, 5, 6, 7);
        b[i] = __builtin_bit_cast(bf16x8, b8);
      }
#pragma unroll
      for (int i = 0; i < 2; ++i)
#pragma unroll
        for (int j = 0; j < 2; ++j)
          acc[i][j] = ISV ? MFMA32(a[i], b[j], acc[i][j]) : MFMA32(b[j], a[i], acc[i][j]);
    }
    if (c + 1 < 8) commit((c + 1) & 1);
    __syncthreads();
  }
#pragma unroll
  for (int j = 0; j < 2; ++j) {
    const int col0 = n0 + wn * 64 + j * 32;
    if (!ISV) {
      char* img = ws + (nt < 4 ? WS_Q : WS_K);
      const int head = (col0 & 511) >> 4;
#pragma unroll
      for (int i = 0; i < 2; ++i) {
        const int tok = m0 + wm * 64 + i * 32 + r31;
#pragma unroll
        for (int hh = 0; hh < 2; ++hh)
          *(bf16x8*)(img + ((size_t)((head + hh) * 2 + h) * SEQ + tok) * 16) = pack8(acc[i][j], hh);
      }
    } else {
      const int head = ((col0 - 1024) >> 4) + (r31 >> 4), d = r31 & 15;
      char* img = ws + WS_V + (size_t)head * VHEAD_BYTES;
#pragma unroll
      for (int i = 0; i < 2; ++i)
#pragma unroll
        for (int gg = 0; gg < 2; ++gg) {
          const int g = ((m0 + wm * 64 + i * 32) >> 4) + gg;
          *(bf16x8*)(img + g * 544 + h * 272 + d * 16) = pack8(acc[i][j], gg);
          if (d == 0) {
            u32x4 ones = {0x3f803f80u, 0x3f803f80u, 0x3f803f80u, 0x3f803f80u};
            *(u32x4*)(img + g * 544 + h * 272 + 256) = ones;
          }
        }
    }
  }
}

DI unsigned ordkey(float rv, int idx) {
  unsigned u = __float_as_uint(rv) >> 16;
  u = (u & 0x8000u) ? (~u & 0xffffu) : (u | 0x8000u);
  return (u << 16) | (unsigned)(2047 - idx);
}
#define DPP_MAXU(v, ctrl, rm) v = max(v, (unsigned)__builtin_amdgcn_update_dpp(0u, v, ctrl, rm, 0xf, true))

__global__ __launch_bounds__(256, 2) void k1_qkv(const unsigned short* __restrict__ wqkv,
                                                 const unsigned short* __restrict__ w_gate,
                                                 const unsigned short* __restrict__ w_up,
                                                 const unsigned short* __restrict__ w_down,
                                                 const unsigned short* __restrict__ wo,
                                                 char* __restrict__ ws) {
  __shared__ __attribute__((aligned(16))) char smem[K1_LDS];
  const int tid = threadIdx.x, lane = tid & 63, wv = tid >> 6;
  const int bid = blockIdx.x;
  if (bid < K1_NGEMM) {
    const int nt = bid % 12, mt = bid / 12;
    if (nt >= 8) k1_gemm_body<true>(wqkv, ws, smem, mt, nt);
    else k1_gemm_body<false>(wqkv, ws, smem, mt, nt);
    return;
  }
  if (bid < K1_NGEMM + NFCH) {
    const int f0 = (bid - K1_NGEMM) * FCH;
    unsigned* wkey = (unsigned*)smem;
    int* sel = (int*)(smem + 64);
    float* xs = (float*)(smem + 256);
    float* red = (float*)(smem + 256 + 16384);
    float* hh = (float*)(smem + 256 + 16384 + 8192);
    unsigned key[8];
    const float* r = (const float*)(ws + WS_R);
#pragma unroll
    for (int i = 0; i < 8; ++i) key[i] = ordkey(r[tid * 8 + i], tid * 8 + i);
    for (int it = 0; it < NSEL; ++it) {
      unsigned b = max(max(max(key[0], key[1]), max(key[2], key[3])), max(max(key[4], key[5]), max(key[6], key[7])));
      DPP_MAXU(b, 0x111, 0xf); DPP_MAXU(b, 0x112, 0xf); DPP_MAXU(b, 0x114, 0xf); DPP_MAXU(b, 0x118, 0xf);
      DPP_MAXU(b, 0x142, 0xa); DPP_MAXU(b, 0x143, 0xc);
      if (lane == 63) wkey[wv] = b;
      __syncthreads();
      unsigned win = max(max(wkey[0], wkey[1]), max(wkey[2], wkey[3]));
      if (tid == 0) sel[it] = 2047 - (int)(win & 0xffffu);
#pragma unroll
      for (int i = 0; i < 8; ++i) key[i] = (key[i] == win) ? 0u : key[i];
      __syncthreads();
    }
    if (bid == K1_NGEMM && tid < NSEL) {
      const int idx = sel[tid];
      ((int*)(ws + WS_SEL))[tid] = idx;
      const float rv = r[idx];
      ((float*)(ws + WS_SEL + 32))[tid] = bfr(1.0f / (1.0f + __expf(-rv)));
    }
    for (int i = tid; i < NSEL * DM / 2; i += 256) {
      int row = i >> 8, k2 = i & 255;
      unsigned v = *(const unsigned*)(ws + WS_XN + ((size_t)sel[row] * DM + k2 * 2) * 2);
      xs[(k2 * 2) * 8 + row] = bflo(v);
      xs[(k2 * 2 + 1) * 8 + row] = bfhi(v);
    }
    __syncthreads();
    {
      const int cp = tid & 7, ks = tid >> 3;
      f32x2 ag[NSEL], au[NSEL];
#pragma unroll
      for (int i = 0; i < NSEL; ++i) { ag[i] = (f32x2){0.f, 0.f}; au[i] = (f32x2){0.f, 0.f}; }
      unsigned g2[16], u2[16];
#pragma unroll
      for (int kk = 0; kk < 16; ++kk) {
        const int k = ks * 16 + kk;
        g2[kk] = *(const unsigned*)(w_gate + (size_t)k * FF + f0 + cp * 2);
        u2[kk] = *(const unsigned*)(w_up + (size_t)k * FF + f0 + cp * 2);
      }
#pragma unroll
      for (int kk = 0; kk < 16; ++kk) {
        const int k = ks * 16 + kk;
        f32x2 g = {bflo(g2[kk]), bfhi(g2[kk])}, u = {bflo(u2[kk]), bfhi(u2[kk])};
        f32x4 xa = *(const f32x4*)(xs + k * 8), xb = *(const f32x4*)(xs + k * 8 + 4);
#pragma unroll
        for (int i = 0; i < 4; ++i) {
          ag[i] += g * xa[i]; au[i] += u * xa[i];
          ag[4 + i] += g * xb[i]; au[4 + i] += u * xb[i];
        }
      }
#pragma unroll
      for (int i = 0; i < NSEL; ++i)
#pragma unroll
        for (int c = 0; c < 2; ++c) {
#pragma unroll
          for (int o = 8; o < 64; o <<= 1) {
            ag[i][c] += __shfl_xor(ag[i][c], o);
            au[i][c] += __shfl_xor(au[i][c], o);
          }
        }
      if (lane < 8) {
#pragma unroll
        for (int i = 0; i < NSEL; ++i)
#pragma unroll
          for (int c = 0; c < 2; ++c) {
            red[((wv * 2 + 0) * NSEL + i) * FCH + cp * 2 + c] = ag[i][c];
            red[((wv * 2 + 1) * NSEL + i) * FCH + cp * 2 + c] = au[i][c];
          }
      }
    }
    __syncthreads();
    if (tid < NSEL * FCH) {
      const int row = tid >> 4, f = tid & 15;
      float g = 0.f, u = 0.f;
#pragma unroll
      for (int w = 0; w < 4; ++w) { g += red[(w * 2 + 0) * NSEL * FCH + tid]; u += red[(w * 2 + 1) * NSEL * FCH + tid]; }
      g = bfr(g); u = bfr(u);
      float sl = bfr(g / (1.0f + __expf(-g)));
      hh[f * 8 + row] = bfr(sl * u);
    }
    __syncthreads();
    {
      const int n2 = tid;
      f32x2 y[NSEL];
#pragma unroll
      for (int i = 0; i < NSEL; ++i) y[i] = (f32x2){0.f, 0.f};
      unsigned wd[FCH];
#pragma unroll
      for (int f = 0; f < FCH; ++f) wd[f] = *(const unsigned*)(w_down + (size_t)(f0 + f) * DM + n2 * 2);
#pragma unroll
      for (int f = 0; f < FCH; ++f) {
        f32x2 w = {bflo(wd[f]), bfhi(wd[f])};
        f32x4 ha = *(const f32x4*)(hh + f * 8), hb = *(const f32x4*)(hh + f * 8 + 4);
#pragma unroll
        for (int i = 0; i < 4; ++i) { y[i] += w * ha[i]; y[4 + i] += w * hb[i]; }
      }
#pragma unroll
      for (int i = 0; i < NSEL; ++i)
        *(f32x2*)((float*)(ws + WS_YP) + ((size_t)(bid - K1_NGEMM) * NSEL + i) * DM + n2 * 2) = y[i];
    }
    return;
  }
  {
    const int tb = bid - K1_NGEMM - NFCH;
    unsigned short* tile = (unsigned short*)smem;
    for (int i = tid; i < 512 * 8; i += 256) {
      int k = i >> 3, c8 = i & 7;
      s16x8 v = *(const s16x8*)(wo + (size_t)k * DM + tb * 64 + c8 * 8);
#pragma unroll
      for (int j = 0; j < 8; ++j) tile[k * 66 + c8 * 8 + j] = (unsigned short)v[j];
    }
    __syncthreads();
    for (int i = tid; i < 64 * 64; i += 256) {
      int n = i >> 6, pc = i & 63;
      int head = pc >> 1, hh2 = pc & 1;
      s16x8 v;
#pragma unroll
      for (int j = 0; j < 8; ++j) v[j] = (short)tile[(head * 16 + 8 * (j >> 2) + 4 * hh2 + (j & 3)) * 66 + n];
      *(s16x8*)(ws + WS_WOT + ((size_t)(tb * 64 + n) * DM + pc * 8) * 2) = v;
    }
  }
}

constexpr int K2_KBYTES = 2 * SEQ * 16;
constexpr int K2_LDS = K2_KBYTES + VHEAD_BYTES + 512;

__global__ __launch_bounds__(1024) void k2_attn(char* __restrict__ ws) {
  __shared__ __attribute__((aligned(16))) char smem[K2_LDS];
  const int tid = threadIdx.x, lane = tid & 63, wv = tid >> 6;
  const int r31 = lane & 31, h = lane >> 5;
  const int ab = blockIdx.x;
  const int head = (ab & 7) * 4 + ((ab >> 3) & 3), qb = ab >> 5;
  char* Kl = smem;
  char* Vl = smem + K2_KBYTES;
  {
    const char* kg = ws + WS_K + (size_t)head * K2_KBYTES;
    const char* vg = ws + WS_V + (size_t)head * VHEAD_BYTES;
    u32x4 t[9];
#pragma unroll
    for (int i = 0; i < 4; ++i) t[i] = *(const u32x4*)(kg + (tid + 1024 * i) * 16);
#pragma unroll
    for (int i = 0; i < 5; ++i) {
      int off = (tid + 1024 * i) * 16;
      if (off < VHEAD_BYTES) t[4 + i] = *(const u32x4*)(vg + off);
    }
#pragma unroll
    for (int i = 0; i < 4; ++i) *(u32x4*)(Kl + (tid + 1024 * i) * 16) = t[i];
#pragma unroll
    for (int i = 0; i < 5; ++i) {
      int off = (tid + 1024 * i) * 16;
      if (off < VHEAD_BYTES) *(u32x4*)(Vl + off) = t[4 + i];
    }
  }
  const int qt = wv & 7, half = wv >> 3;
  const int q0 = qb * 256 + qt * 32;
  const bf16x8 qf = *(const bf16x8*)(ws + WS_Q + ((size_t)(head * 2 + h) * SEQ + q0 + r31) * 16);
  __syncthreads();

  const float cs = 0.25f * 1.4426950408889634f;
  f32x16 o;
#pragma unroll
  for (int i = 0; i < 16; ++i) o[i] = 0.f;
  float m = -INFINITY;
  f32x16 zero;
#pragma unroll
  for (int i = 0; i < 16; ++i) zero[i] = 0.f;

  for (int grp = 0; grp < 8; ++grp) {
    const int key0 = half * 1024 + grp * 128;
    f32x16 s[4];
#pragma unroll
    for (int t = 0; t < 4; ++t) {
      bf16x8 kf = *(const bf16x8*)(Kl + h * 32768 + (key0 + t * 32 + r31) * 16);
      s[t] = MFMA32(kf, qf, zero);
    }
    float mx = s[0][0];
#pragma unroll
    for (int t = 0; t < 4; ++t)
#pragma unroll
      for (int i = 0; i < 16; ++i) mx = fmaxf(mx, s[t][i]);
    {
      auto sw = __builtin_amdgcn_permlane32_swap(__float_as_uint(mx), __float_as_uint(mx), false, false);
      mx = fmaxf(__uint_as_float(sw[0]), __uint_as_float(sw[1]));
    }
    mx = bfr(mx);
    const float mnew = fmaxf(m, mx);
    const float alpha = __builtin_amdgcn_exp2f((m - mnew) * cs);
    m = mnew;
    const float nm = -mnew * cs;
    const f32x2 cs2 = {cs, cs}, nm2 = {nm, nm};
#pragma unroll
    for (int i = 0; i < 9; ++i) o[i] *= alpha;
#pragma unroll
    for (int t = 0; t < 4; ++t) {
      f32x16 p;
#pragma unroll
      for (int i = 0; i < 16; i += 2) {
        f32x2 sv = {bfr(s[t][i]), bfr(s[t][i + 1])};
        f32x2 tv = sv * cs2 + nm2;
        p[i] = __builtin_amdgcn_exp2f(tv[0]);
        p[i + 1] = __builtin_amdgcn_exp2f(tv[1]);
      }
      const int g = (key0 + t * 32) >> 4;
      bf16x8 v0 = *(const bf16x8*)(Vl + g * 544 + h * 272 + r31 * 16);
      bf16x8 v1 = *(const bf16x8*)(Vl + (g + 1) * 544 + h * 272 + r31 * 16);
      o = MFMA32(v0, pack8(p, 0), o);
      o = MFMA32(v1, pack8(p, 1), o);
    }
  }
  __syncthreads();
  float* mb = (float*)smem;
  if (half == 1) {
#pragma unroll
    for (int i = 0; i < 9; ++i) mb[(qt * 10 + i) * 64 + lane] = o[i];
    mb[(qt * 10 + 9) * 64 + lane] = m;
  }
  __syncthreads();
  if (half == 0) {
    const float m2 = mb[(qt * 10 + 9) * 64 + lane];
    const float mm = fmaxf(m, m2);
    const float a1 = __builtin_amdgcn_exp2f((m - mm) * cs), a2 = __builtin_amdgcn_exp2f((m2 - mm) * cs);
#pragma unroll
    for (int i = 0; i < 9; ++i) o[i] = o[i] * a1 + mb[(qt * 10 + i) * 64 + lane] * a2;
    auto sw = __builtin_amdgcn_permlane32_swap(__float_as_uint(o[8]), __float_as_uint(o[8]), false, false);
    const float l = __uint_as_float(sw[0]);
    const float inv = 1.0f / l;
#pragma unroll
    for (int i = 0; i < 8; ++i) o[i] *= inv;
    bf16x8 ov = pack8(o, 0);
    *(bf16x8*)(ws + WS_O + ((size_t)(q0 + r31) * DM + head * 16 + h * 8) * 2) = ov;
  }
}

constexpr int K3_P = 1040;
__global__ __launch_bounds__(256) void k3_out(const unsigned short* __restrict__ x,
                                              const char* __restrict__ ws,
                                              unsigned short* __restrict__ out) {
  __shared__ __attribute__((aligned(16))) char smem[2 * 64 * K3_P + 256 + 8 * 4 * 64 * 4];
  int* slot = (int*)(smem + 2 * 64 * K3_P);
  float* ypart = (float*)(smem + 2 * 64 * K3_P + 256);
  const int tid = threadIdx.x, lane = tid & 63, wv = tid >> 6;
  const int r31 = lane & 31, h = lane >> 5;
  const int mt = blockIdx.x >> 3, nt = blockIdx.x & 7;
  {
    const char* Ag = ws + WS_O + (size_t)mt * 64 * DM * 2;
    const char* Bg = ws + WS_WOT + (size_t)nt * 64 * DM * 2;
#pragma unroll
    for (int half = 0; half < 2; ++half) {
      u32x4 ta[8], tb[8];
#pragma unroll
      for (int i = 0; i < 8; ++i) {
        int p = tid + 256 * (i + 8 * half);
        ta[i] = *(const u32x4*)(Ag + p * 16);
        tb[i] = *(const u32x4*)(Bg + p * 16);
      }
#pragma unroll
      for (int i = 0; i < 8; ++i) {
        int p = tid + 256 * (i + 8 * half);
        *(u32x4*)(smem + (p >> 6) * K3_P + (p & 63) * 16) = ta[i];
        *(u32x4*)(smem + 64 * K3_P + (p >> 6) * K3_P + (p & 63) * 16) = tb[i];
      }
    }
  }
  if (tid < 64) slot[tid] = -1;
  __syncthreads();
  const int* selg = (const int*)(ws + WS_SEL);
  if (tid < NSEL) { int idx = selg[tid]; if ((idx >> 6) == mt) slot[idx & 63] = tid; }
  for (int sidx = 0; sidx < NSEL; ++sidx) {
    if ((selg[sidx] >> 6) == mt) {
      const float* yp = (const float*)(ws + WS_YP) + (size_t)sidx * DM + nt * 64 + (tid & 63);
      float y = 0.f;
#pragma unroll 11
      for (int c = (tid >> 6); c < NFCH; c += 4) y += yp[(size_t)c * NSEL * DM];
      ypart[(sidx * 4 + (tid >> 6)) * 64 + (tid & 63)] = y;
    }
  }
  __syncthreads();
  const char* Ab = smem + ((wv >> 1) * 32 + r31) * K3_P + 16 * h;
  const char* Bb = smem + 64 * K3_P + ((wv & 1) * 32 + r31) * K3_P + 16 * h;
  f32x16 acc;
#pragma unroll
  for (int i = 0; i < 16; ++i) acc[i] = 0.f;
#pragma unroll 8
  for (int ks = 0; ks < 32; ++ks) {
    bf16x8 a = *(const bf16x8*)(Ab + ks * 32);
    bf16x8 b = *(const bf16x8*)(Bb + ks * 32);
    acc = MFMA32(a, b, acc);
  }
  const int n = nt * 64 + (wv & 1) * 32 + r31;
  const unsigned short* xn = (const unsigned short*)(ws + WS_XN);
#pragma unroll
  for (int i = 0; i < 16; ++i) {
    const int lr = (wv >> 1) * 32 + (i & 3) + 8 * (i >> 2) + 4 * h;
    const int tok = mt * 64 + lr;
    const float xa = bfr(bf2f(x[(size_t)tok * DM + n]) + bfr(acc[i]));
    float b = bf2f(xn[(size_t)tok * DM + n]);
    const int sl = slot[lr];
    if (sl >= 0) {
      const int cn = (wv & 1) * 32 + r31;
      float y = ypart[(sl * 4 + 0) * 64 + cn] + ypart[(sl * 4 + 1) * 64 + cn] + ypart[(sl * 4 + 2) * 64 + cn] + ypart[(sl * 4 + 3) * 64 + cn];
      const float sg = ((const float*)(ws + WS_SEL + 32))[sl];
      b = bfr(b + bfr(bfr(y) * sg));
    }
    out[(size_t)tok * DM + n] = f2bf(xa + b);
  }
}

extern "C" void kernel_launch(void* const* d_in, const int* in_sizes, int n_in,
                              void* d_out, int out_size, void* d_ws, size_t ws_size,
                              hipStream_t stream) {
  const unsigned short* x = (const unsigned short*)d_in[0];
  const unsigned short* wqkv = (const unsigned short*)d_in[1];
  const unsigned short* wo = (const unsigned short*)d_in[2];
  const unsigned short* w_r = (const unsigned short*)d_in[4];
  const unsigned short* w_gate = (const unsigned short*)d_in[5];
  const unsigned short* w_up = (const unsigned short*)d_in[6];
  const unsigned short* w_down = (const unsigned short*)d_in[7];
  char* ws = (char*)d_ws;
  k0_ln<<<256, 256, 0, stream>>>(x, w_r, ws);
  k1_qkv<<<K1_NGEMM + NFCH + 8, 256, 0, stream>>>(wqkv, w_gate, w_up, w_down, wo, ws);
  k2_attn<<<256, 1024, 0, stream>>>(ws);
  k3_out<<<256, 256, 0, stream>>>(x, ws, (unsigned short*)d_out);
}
```
